```python
import torch

def _softmax(x, axis=-1):
    return torch.softmax(x, dim=axis)
def _gelu(x):
    return torch.nn.functional.gelu(x, approximate="tanh")
def _silu(x):
    return torch.nn.functional.silu(x)
def _sigmoid(x):
    return torch.sigmoid(x)
def _layernorm(x, axis=-1, eps=1e-5):
    m = x.mean(dim=axis, keepdim=True)
    v = x.var(dim=axis, keepdim=True, unbiased=False)
    return (x - m) / torch.sqrt(v + eps)
def _scatter_add(tgt, idx, src, axis=0):
    return tgt.clone().index_add_(axis, idx, src)
def _segment_sum(src, seg, n):
    out = torch.zeros((n,) + src.shape[1:], dtype=src.dtype)
    return out.index_add_(0, seg, src)
def _pad(x, pw):
    flat = tuple(v for pair in reversed(pw) for v in pair)
    return torch.nn.functional.pad(x, flat)

kernel_name = 'concat_heads__linear_kernel_hedgehog__ssm_conv_s4__prompt'

def setup_inputs():

    _w = (lambda a: (lambda t: t.to(torch.bfloat16) if t.is_floating_point() else t)(torch.as_tensor(a)))
    def _A_setup():
        B, S, D, H = 1, 2048, 512, 32
        Dh = D // H
        return {
            "x": _w((lambda *s: torch.randn(*s, dtype=torch.bfloat16) * 0.4)(B, S, D)),
            "wqkv": _w((lambda *s: torch.randn(*s, dtype=torch.bfloat16) * 0.4)(D, 3 * D)),
            "w_phi": _w((lambda *s: torch.randn(*s, dtype=torch.bfloat16) * 0.4)(H, Dh, Dh) * (Dh ** -0.5)),
            "wo": _w((lambda *s: torch.randn(*s, dtype=torch.bfloat16) * 0.05)(D, D)),
        }
    _A_in = _A_setup()

    def _B_setup():
        B, L, D, N = 1, 2048, 512, 128
        A_re = -(lambda _x: torch.exp(torch.as_tensor(_x)))((lambda *s: torch.randn(*s, dtype=torch.bfloat16) * 6.4)(D, N))
        A_im = torch.pi * (lambda *s: torch.randn(*s, dtype=torch.bfloat16) * 0.1)(D, N)
        B_re = (lambda *s: torch.randn(*s, dtype=torch.bfloat16) * 0.2)(D, N)
        B_im = (lambda *s: torch.randn(*s, dtype=torch.bfloat16) * 0.2)(D, N)
        C_re = (lambda *s: torch.randn(*s, dtype=torch.bfloat16) * 0.2)(D, N)
        C_im = (lambda *s: torch.randn(*s, dtype=torch.bfloat16) * 0.2)(D, N)
        log_dt = (lambda *s: torch.randn(*s, dtype=torch.bfloat16) * 0.1)(D) * 0.5 - 4.0
        Dskip = (lambda *s: torch.randn(*s, dtype=torch.bfloat16) * 0.1)(D)
        return {
            "u": _w((lambda *s: torch.randn(*s, dtype=torch.bfloat16) * 0.05)(B, L, D)),
            "A_re": _w(A_re),
            "A_im": _w(A_im),
            "B_re": _w(B_re),
            "B_im": _w(B_im),
            "C_re": _w(C_re),
            "C_im": _w(C_im),
            "log_dt": _w(log_dt),
            "Dskip": _w(Dskip),
        }
    _B_in = _B_setup()

    return {
        "A_x": _A_in["x"],
        "A_wqkv": _A_in["wqkv"],
        "A_w_phi": _A_in["w_phi"],
        "A_wo": _A_in["wo"],
        "B_u": _B_in["u"],
        "B_A_re": _B_in["A_re"],
        "B_A_im": _B_in["A_im"],
        "B_B_re": _B_in["B_re"],
        "B_B_im": _B_in["B_im"],
        "B_C_re": _B_in["C_re"],
        "B_C_im": _B_in["C_im"],
        "B_log_dt": _B_in["log_dt"],
        "B_Dskip": _B_in["Dskip"],
    }

def reference(A_x, A_wqkv, A_w_phi, A_wo, B_u, B_A_re, B_A_im, B_B_re, B_B_im, B_C_re, B_C_im, B_log_dt, B_Dskip):
    def _A_ref(x, wqkv, w_phi, wo):
        B, S, D = x.shape
        H = 32; Dh = D // H
        qkv = (lambda x, *d: x.permute(*d))((x @ wqkv).reshape(B, S, 3, H, Dh), 2, 0, 3, 1, 4)
        q, k, v = qkv[0], qkv[1], qkv[2]
        pq = torch.einsum("bhsd,hdf->bhsf", q, w_phi)
        pk = torch.einsum("bhsd,hdf->bhsf", k, w_phi)
        phi_q = torch.concatenate([_softmax(pq, axis=-1), _softmax(-pq, axis=-1)], axis=-1)
        phi_k = torch.concatenate([_softmax(pk, axis=-1), _softmax(-pk, axis=-1)], axis=-1)
        kv = torch.einsum("bhsf,bhsd->bhfd", phi_k, v)
        z  = torch.einsum("bhsf->bhf", phi_k)
        num = torch.einsum("bhqf,bhfd->bhqd", phi_q, kv)
        den = torch.einsum("bhqf,bhf->bhq", phi_q, z)[..., None] + 1e-6
        o = num / den
        return (lambda x, *d: x.permute(*d))(o, 0, 2, 1, 3).reshape(B, S, D) @ wo

    def _B_ref(u, A_re, A_im, B_re, B_im, C_re, C_im, log_dt, Dskip):
        Bsz, L, D = u.shape
        dt = torch.exp(log_dt)[:, None]
        A = A_re + 1j * A_im
        Bc = B_re + 1j * B_im
        Cc = C_re + 1j * C_im
        logdA = dt * A
        dB = dt * Bc
        l = (lambda x, dt: x.to(dt))(torch.arange(L), A_re.dtype)
        P = torch.exp(logdA[:, :, None] * l[None, None, :])
        K = torch.einsum("dn,dnl,dn->dl", Cc, P, dB).real
        L2 = 2 * L
        Kp = _pad(K, ((0, 0), (0, L)))
        up = (lambda x, *d: x.permute(*d))(u, 0, 2, 1)
        up = _pad(up, ((0, 0), (0, 0), (0, L)))
        Kf = (lambda x, axes=(-2,-1): torch.fft.rfft2(x.float(), dim=axes))(Kp[:, None, :], axes=(-2, -1))
        Uf = (lambda x, axes=(-2,-1): torch.fft.rfft2(x.float(), dim=axes))(up[:, :, None, :], axes=(-2, -1))
        Yf = Uf * Kf[None, :, :, :]
        y = (lambda x, s, axes=(-2,-1): torch.fft.irfft2(x, s=s, dim=axes).to(torch.bfloat16))(Yf, (1, L2), axes=(-2, -1))[:, :, 0, :L]
        y = (lambda x, *d: x.permute(*d))(y, 0, 2, 1) + u * Dskip[None, None, :]
        return y

    a = _A_ref(A_x, A_wqkv, A_w_phi, A_wo)
    b = _B_ref(B_u, B_A_re, B_A_im, B_B_re, B_B_im, B_C_re, B_C_im, B_log_dt, B_Dskip)
    return torch.concatenate([a, b], axis=-1)
```

```cpp
#include <hip/hip_runtime.h>
#include <stdint.h>

typedef unsigned int u32;
typedef unsigned long long u64;
typedef __attribute__((ext_vector_type(2))) float f32x2;
typedef __attribute__((ext_vector_type(4))) float f32x4;
typedef __attribute__((ext_vector_type(16))) float f32x16;
typedef __attribute__((ext_vector_type(2))) u32 u32x2;
typedef __attribute__((ext_vector_type(4))) u32 u32x4;
typedef __attribute__((ext_vector_type(4))) short s16x4;
typedef __attribute__((ext_vector_type(8))) __bf16 bf16x8;
typedef __attribute__((ext_vector_type(2))) __bf16 bf16x2;
#define LAS __attribute__((address_space(3)))
#if defined(__HIP_DEVICE_COMPILE__)
#define NOPK __attribute__((target("no-packed-fp32-ops")))
#else
#define NOPK
#endif
#define DI __device__ __forceinline__ NOPK
typedef LAS char* lptr;

#define WS_EPOCH_A 0
#define WS_EPOCH_B 256
#define WS_F1 (32 * 1024)
#define WS_FU (96 * 1024)
#define WS_KVP (256 * 1024)
#define WS_OX (2 * 1024 * 1024)
#define WS_UT (4 * 1024 * 1024)

#define SPIN_CAP (1u << 18)
#define SENT 0xffffffffu
#define PER 5

struct Params {
  const char* x; const char* wqkv; const char* wphi; const char* wo;
  const char* u; const char* are; const char* aim; const char* bre; const char* bim;
  const char* cre; const char* cim; const char* ldt; const char* dsk;
  char* out; char* ws;
};

struct Touch { const char* a[32]; };

DI u32x4 lds_r128(lptr p) { return *(LAS u32x4*)p; }
DI void lds_w128(lptr p, u32x4 v) { *(LAS u32x4*)p = v; }
DI void lds_w64(lptr p, u32x2 v) { *(LAS u32x2*)p = v; }
DI void lds_w32(lptr p, u32 v) { *(LAS u32*)p = v; }
DI u32 lds_r32(lptr p) { return *(LAS u32*)p; }
DI u32x2 lds_tr(lptr p) { return __builtin_bit_cast(u32x2, __builtin_amdgcn_ds_read_tr16_b64_v4i16((LAS s16x4*)p)); }
DI bf16x8 mk8(u32x2 a, u32x2 b) { u32x4 v = {a.x, a.y, b.x, b.y}; return __builtin_bit_cast(bf16x8, v); }
DI bf16x8 tr8(lptr p, int d) { return mk8(lds_tr(p), lds_tr(p + d)); }
DI bf16x8 as8(u32x4 v) { return __builtin_bit_cast(bf16x8, v); }
DI u32 pk2(float a, float b) { f32x2 v = {a, b}; return __builtin_bit_cast(u32, __builtin_convertvector(v, bf16x2)); }
DI float bflo(u32 v) { return __builtin_bit_cast(float, v << 16); }
DI float bfhi(u32 v) { return __builtin_bit_cast(float, v & 0xffff0000u); }
#define MFMA32(a, b, c) __builtin_amdgcn_mfma_f32_32x32x16_bf16((a), (b), (c), 0, 0, 0)
#define MFMA16(a, b, c) __builtin_amdgcn_mfma_f32_16x16x32_bf16((a), (b), (c), 0, 0, 0)
#define CFENCE() asm volatile("" ::: "memory")
#define SB() __builtin_amdgcn_sched_barrier(0)
#define PIN(a) do { asm volatile("" : "+v"(a)); __builtin_amdgcn_sched_barrier(0); } while (0)
#define BARRIER() do { asm volatile("s_waitcnt lgkmcnt(0)\n\ts_barrier" ::: "memory"); } while (0)
template <int N> DI void vmwait() { asm volatile("s_waitcnt vmcnt(%0)" ::"i"(N) : "memory"); }

template <int OFF> DI void gload16_nt(u32x4& d, const char* sbase, u32 voff) {
  asm volatile("global_load_dwordx4 %0, %1, %2 offset:%3 nt" : "=v"(d) : "v"(voff), "s"(sbase), "i"(OFF) : "memory");
}
DI void glds16(const char* sbase, u32 voff, u32 lds) {
  asm volatile("s_mov_b32 m0, %2\n\ts_nop 0\n\tglobal_load_lds_dwordx4 %0, %1" ::"v"(voff), "s"(sbase), "s"(lds) : "memory");
}
template <int OFF> DI void glds16o(const char* sbase, u32 voff, u32 lds) {
  asm volatile("s_mov_b32 m0, %2\n\ts_nop 0\n\tglobal_load_lds_dwordx4 %0, %1 offset:%3" ::"v"(voff), "s"(sbase), "s"(lds), "i"(OFF) : "memory");
}
template <int OFF> DI void gload16(u32x4& d, const char* sbase, u32 voff) {
  asm volatile("global_load_dwordx4 %0, %1, %2 offset:%3" : "=v"(d) : "v"(voff), "s"(sbase), "i"(OFF) : "memory");
}
template <int OFF> DI void gload16_sc1(u32x4& d, const char* sbase, u32 voff) {
  asm volatile("global_load_dwordx4 %0, %1, %2 offset:%3 sc1" : "=v"(d) : "v"(voff), "s"(sbase), "i"(OFF) : "memory");
}
template <int OFF> DI void gload8_sc1(u32x2& d, const char* sbase, u32 voff) {
  asm volatile("global_load_dwordx2 %0, %1, %2 offset:%3 sc1" : "=v"(d) : "v"(voff), "s"(sbase), "i"(OFF) : "memory");
}
DI void gload4(u32& d, const char* sbase, u32 voff) {
  asm volatile("global_load_dword %0, %1, %2" : "=v"(d) : "v"(voff), "s"(sbase) : "memory");
}
DI void gload4_sc1(u32& d, const char* sbase, u32 voff) {
  asm volatile("global_load_dword %0, %1, %2 sc1" : "=v"(d) : "v"(voff), "s"(sbase) : "memory");
}
DI void gload2_nt(u32& d, const char* sbase, u32 voff) {
  asm volatile("global_load_ushort %0, %1, %2 nt" : "=v"(d) : "v"(voff), "s"(sbase) : "memory");
}
template <int OFF> DI void gstore16_sc1(char* sbase, u32 voff, u32x4 v) {
  asm volatile("global_store_dwordx4 %0, %1, %2 offset:%3 sc1\n\ts_nop 1" ::"v"(voff), "v"(v), "s"(sbase), "i"(OFF) : "memory");
}
template <int OFF> DI void gstore16_out(char* sbase, u32 voff, u32x4 v) {
  asm volatile("global_store_dwordx4 %0, %1, %2 offset:%3 sc1 nt\n\ts_nop 1" ::"v"(voff), "v"(v), "s"(sbase), "i"(OFF) : "memory");
}
template <int OFF> DI void gstore8_sc1(char* sbase, u32 voff, u32x2 v) {
  asm volatile("global_store_dwordx2 %0, %1, %2 offset:%3 sc1" ::"v"(voff), "v"(v), "s"(sbase), "i"(OFF) : "memory");
}
DI void gstore4_sc1(char* sbase, u32 voff, u32 v) {
  asm volatile("global_store_dword %0, %1, %2 sc1" ::"v"(voff), "v"(v), "s"(sbase) : "memory");
}
DI void put_flag(char* line, int tid, u32 tag) {
  if (tid >= 248) { u32x4 t = {tag, tag, tag, tag}; gstore16_sc1<0>(line, (u32)(tid - 248) * 16, t); }
}
DI void poll_flags(const char* fbase, int lane, int nmask, u32 tag) {
  const u32 voff = (u32)lane * 128;
  u32 v = lane <= nmask ? ~tag : tag;
#pragma unroll 1
  for (u32 spins = 0; spins < SPIN_CAP; ++spins) {
    if (v != tag) asm volatile("global_load_dword %0, %1, %2 sc1\n\ts_waitcnt vmcnt(0)" : "=v"(v) : "v"(voff), "s"(fbase) : "memory");
    if (__builtin_amdgcn_ballot_w64(v != tag) == 0) break;
  }
}

template <class F> DI void feature_map(const f32x16& Pf, u32 (&o)[16], F hook) {
  const float L = 1.4426950408889634f;
  float P[16];
  float mx, mn;
#pragma unroll
  for (int i = 0; i < 16; i += 2) {
    const u32 r = pk2(Pf[i], Pf[i + 1]);
    P[i] = bflo(r); P[i + 1] = bfhi(r);
    mx = i ? __builtin_fmaxf(__builtin_fmaxf(mx, P[i]), P[i + 1]) : __builtin_fmaxf(P[0], P[1]);
    mn = i ? __builtin_fminf(__builtin_fminf(mn, P[i]), P[i + 1]) : __builtin_fminf(P[0], P[1]);
    if ((i & 3) == 2) hook(4 + (i >> 2), mx, mn);
  }
  const float mxl = -mx * L, mnl = mn * L;
  float ep[16], en[16];
  float sp = 0.f, sn = 0.f;
#pragma unroll
  for (int i = 0; i < 16; ++i) {
    ep[i] = __builtin_amdgcn_exp2f(__builtin_fmaf(P[i], L, mxl));
    en[i] = __builtin_amdgcn_exp2f(__builtin_fmaf(P[i], -L, mnl));
    if (i == 1) { sp = ep[0]; sn = en[0]; }
    if (i > 1) { sp += ep[i - 1]; sn += en[i - 1]; asm volatile("" : "+v"(sp), "+v"(sn)); }
    hook(8 + i, ep[i], en[i]);
  }
  const float rp = __builtin_amdgcn_rcpf(sp + ep[15]), rn = __builtin_amdgcn_rcpf(sn + en[15]);
#pragma unroll
  for (int i = 0; i < 8; ++i) {
    o[i] = pk2(ep[2 * i] * rp, ep[2 * i + 1] * rp);
    o[8 + i] = pk2(en[2 * i] * rn, en[2 * i + 1] * rn);
    hook(24 + i, o[i], o[8 + i]);
  }
}
DI bf16x8 cvt8(const f32x16& a, int s) {
  u32x4 v = {pk2(a[8 * s], a[8 * s + 1]), pk2(a[8 * s + 2], a[8 * s + 3]), pk2(a[8 * s + 4], a[8 * s + 5]), pk2(a[8 * s + 6], a[8 * s + 7])};
  return as8(v);
}
DI float row_sum16(float v) {
  v += __builtin_bit_cast(float, __builtin_amdgcn_update_dpp(0, __builtin_bit_cast(int, v), 0x128, 0xf, 0xf, false));
  v += __builtin_bit_cast(float, __builtin_amdgcn_update_dpp(0, __builtin_bit_cast(int, v), 0x124, 0xf, 0xf, false));
  v += __builtin_bit_cast(float, __builtin_amdgcn_update_dpp(0, __builtin_bit_cast(int, v), 0x122, 0xf, 0xf, false));
  v += __builtin_bit_cast(float, __builtin_amdgcn_update_dpp(0, __builtin_bit_cast(int, v), 0x121, 0xf, 0xf, false));
  return v;
}
#define RAWBAR() asm volatile("s_barrier" ::: "memory")

#define A_PH 65536
#define A_VB 32768
#define A_SC 16384
#define WO_AT(it) ((((it) + 8) % 20) * 4096)

#define CND(a, b, c, PERM) "v_cndmask_b32_dpp %" #a ", %" #b ", %" #c ", vcc " PERM " row_mask:0xf bank_mask:0xf bound_ctrl:1\n\t"
#define MVD(a, b, PERM) "v_mov_b32_dpp %" #a ", %" #b " " PERM " row_mask:0xf bank_mask:0xf bound_ctrl:1\n\t"
#define BFK(a, k) "v_bfi_b32 %" #a ", %25, %" #k ", %" #a "\n\t"
#define BFO(a, k) "v_bfi_b32 %" #a ", %25, %" #a ", %" #k "\n\t"
#define SEL8(d0, k0, o0, d1, k1, o1, mask, vm, BF, PERM) \
  asm volatile("s_mov_b64 vcc, %24\n\t" \
               CND(0, 12, 8, PERM) MVD(5, 21, PERM) CND(1, 13, 9, PERM) MVD(6, 22, PERM) CND(2, 14, 10, PERM) MVD(7, 23, PERM) \
               CND(3, 15, 11, PERM) BF(5, 17) CND(4, 20, 16, PERM) BF(6, 18) BF(7, 19) \
               : "=&v"(d0[0]), "=&v"(d0[1]), "=&v"(d0[2]), "=&v"(d0[3]), "=&v"(d1[0]), "=&v"(d1[1]), "=&v"(d1[2]), "=&v"(d1[3]) \
               : "v"(k0[0]), "v"(k0[1]), "v"(k0[2]), "v"(k0[3]), "v"(o0[0]), "v"(o0[1]), "v"(o0[2]), "v"(o0[3]), \
                 "v"(k1[0]), "v"(k1[1]), "v"(k1[2]), "v"(k1[3]), "v"(o1[0]), "v"(o1[1]), "v"(o1[2]), "v"(o1[3]), "s"(mask), "v"(vm) : "vcc")

constexpr int kwait(int c) {
  int issued = 18 + PER * (c + 1); if (issued > 53) issued = 53;
  int pos = c < 3 ? 13 + 4 * c : 17 + 4 * c;
  return issued - 1 - pos;
}
DI u32 umax3(u32 a, u32 b, u32 c) { return __builtin_elementwise_max(__builtin_elementwise_max(a, b), c); }
DI u32 nosent(u32 a) { return __builtin_elementwise_min(a, 0xfffffffeu); }

DI void role_a(Params p, lptr sm, int b, u32 pf1) {
  const int tid = __builtin_amdgcn_workitem_id_x(), l = tid & 63;
  const int w = __builtin_amdgcn_readfirstlane(tid >> 6);
  const int xcd = b & 7, ii = b >> 3;
  const int rb = (xcd & 3) * 4 + (ii & 3), cb = (xcd >> 2) * 8 + (ii >> 2);
  const int rot = (ii >> 2) + 2 * (ii & 3);
  const u32 smb = (u32)(uintptr_t)sm + w * 1024;
  __builtin_amdgcn_s_setprio(2);

  u32 epoch; u32x4 wphi_raw;
  const u32 xvoff = (u32)(((l >> 2) & 7) * 4096 + (l >> 5) * 64 + (l & 3) * 16);
  const u32 wvoff = (u32)((w * 16 + (l >> 2)) * 3072 + cb * 64 + (l & 3) * 16);
  const char* xb = p.x + (size_t)(rb * 128 + w * 32) * 1024;
  const int rt = rb * 8 + (cb >> 3) * 4 + w;
  char* kb = p.ws + WS_KVP + ((cb * 4 + w) * 16 + rb) * 640;
  char* ob = p.ws + WS_OX + ((rb * 8 + w * 2) * 16 + cb) * 1024;
  u32x4 X[8][4];
  const char* xc[8]; const char* wc[8];
#pragma unroll
  for (int c = 0; c < 8; ++c) { xc[c] = xb + ((c + rot) & 7) * 128; wc[c] = p.wqkv + ((c + rot) & 7) * 196608; }
#define LDX(c, j) gload16<(j) * 1024>(X[c][j], xc[c], xvoff)
#define LDW(c, m, r) glds16o<(m) * 1024>(wc[c], wvoff, smb + 4096 + (r) + (c) * 4096 - (m) * 1024)
#define LDX4(c) do { LDX(c, 0); LDX(c, 1); LDX(c, 2); LDX(c, 3); } while (0)
#define SEQ(j) do { if ((j) < 12) { if ((j) % 6 < 4) LDX((3 + (j) / 6) & 7, ((j) % 6) & 3); else LDW((2 * ((j) / 6) + (j) % 6) & 7, 1, -4096); } \
                    else if ((j) < 24) LDX((2 + (j) / 4) & 7, (j) & 3); \
                    else if ((j) < 32) LDW(((j) - 24) & 7, 2, 32768 - 4096); \
                    else if ((j) == 32) gstore16_sc1<0>(kb, (u32)(l < 40 ? l : 39) * 16, (u32x4){SENT, SENT, SENT, SENT}); \
                    else if ((j) == 33) gstore16_sc1<0>(ob, l * 16, (u32x4){SENT, SENT, SENT, SENT}); \
                    else if ((j) == 34) gstore16_sc1<0>(ob + 16384, l * 16, (u32x4){SENT, SENT, SENT, SENT}); } while (0)
#define SEQ0(g) (PER * ((g) >> 2) + (PER * ((g) & 3) + 3) / 4)
#define SEQG(g) do { SEQ(SEQ0(g)); if (SEQ0((g) + 1) - SEQ0(g) > 1) SEQ(SEQ0(g) + 1); } while (0)
  glds16(wc[0] + 1024, wvoff, smb);
  LDX4(0); LDW(1, 1, -4096); LDW(2, 1, -4096); LDW(3, 1, -4096);
  gload16<0>(wphi_raw, p.wphi + cb * 1024, l * 16);
  gload4_sc1(epoch, p.ws + WS_EPOCH_A, 0);
  LDX4(1); LDX4(2);
  asm volatile("" : "+s"(xc[3]), "+s"(xc[4]), "+s"(xc[5]), "+s"(xc[6]), "+s"(xc[7]), "+s"(wc[4]), "+s"(wc[5]), "+s"(wc[6]), "+s"(wc[7]));

  const int r32 = l & 31, h = l >> 5, G = l >> 4, q4 = (l >> 2) & 3, p4 = l & 3;
  lptr xs = sm + A_PH + w * 4096;
  lptr wf = sm + (32 * h + q4) * 64 + (G & 1) * 32 + p4 * 8;
#define WF(base, i) tr8(wf + (base) + ((i) >> 2) * 4096 + ((i) & 3) * 512, 256)
  const u64 m1e = 0x5555555555555555ull, m1o = 0xaaaaaaaaaaaaaaaaull, m2e = 0x3333333333333333ull, m2o = 0xccccccccccccccccull;
  const u32 vm1 = (u32)(-(l & 1)), vm2 = (u32)(-((l >> 1) & 1));
  const int hh = w >> 1, sg = w & 1;
  const int swz = ((r32 >> 1) & 3) ^ (((r32 >> 3) & 1) << 1);
  lptr pb = xs + h * 2048 + r32 * 64 + swz * 16;
  lptr vb = sm + A_VB + (w * 32 + r32) * 64 + h * 8 + swz * 16;
  const int sw0 = (q4 >> 1) ^ ((G & 1) << 1);
  const int ro = (8 * G + q4) * 64 + (p4 & 1) * 8;
  lptr pa0 = sm + A_PH + hh * 2048 + ro + (((sg * 2 + (p4 >> 1)) ^ sw0) * 16);
  lptr va0 = sm + A_VB + ro + (((hh * 2 + (p4 >> 1)) ^ sw0) * 16);
  u32 fvo0 = (u32)(((l & 32) ? rb * 16 + ((l >> 1) & 15) : ((l >> 1) & 15) * 16 + cb) * 128);
  asm volatile("" : "+v"(pb), "+v"(vb), "+v"(pa0), "+v"(va0), "+v"(fvo0), "+v"(wf));
#define LXOR(a, k) ((lptr)(uintptr_t)((u32)(uintptr_t)(a) ^ (u32)(k)))

  f32x16 zero16;
#pragma unroll
  for (int i = 0; i < 16; ++i) zero16[i] = 0.f;
  f32x16 accK = zero16, accV = zero16;
  u32x4 T[4];
#define XWAIT(c, N) asm volatile("s_waitcnt vmcnt(%4)" : "+v"(X[c][0]), "+v"(X[c][1]), "+v"(X[c][2]), "+v"(X[c][3]) : "i"(N) : "memory")
#define XT(c, n) do { \
    if ((n) == 0) SEL8(T[0], X[c][0], X[c][1], T[2], X[c][2], X[c][3], m1e, vm1, BFO, "quad_perm:[1,0,3,2]"); \
    if ((n) == 1) SEL8(T[1], X[c][1], X[c][0], T[3], X[c][3], X[c][2], m1o, vm1, BFK, "quad_perm:[1,0,3,2]"); \
    if ((n) == 2) SEL8(X[c][0], T[0], T[2], X[c][1], T[1], T[3], m2e, vm2, BFO, "quad_perm:[2,3,0,1]"); \
    if ((n) == 3) SEL8(X[c][2], T[2], T[0], X[c][3], T[3], T[1], m2o, vm2, BFK, "quad_perm:[2,3,0,1]"); } while (0)
  XWAIT(0, 13);
  asm volatile("" :: "v"(pf1));
#define PRE(n) do { XT(0, n); SEQG(n); SB(); } while (0)
  PRE(0); PRE(1);
  asm volatile("s_waitcnt vmcnt(%2)" : "+v"(wphi_raw), "+v"(epoch) : "i"(8 + SEQ0(2)) : "memory");
  RAWBAR();
  bf16x8 fw[4];
#pragma unroll
  for (int i = 0; i < 4; ++i) fw[i] = WF(0, i);
  lds_w128(xs + l * 16, wphi_raw);
  SB();
  PRE(2); PRE(3);
  u32x4 wpv;
  {
    lptr a = xs + (p4 & 1) * 512 + (4 * h + q4) * 32 + (2 * (G & 1) + (p4 >> 1)) * 8;
    u32x2 lo = lds_tr(a), hi = lds_tr(a + 256);
    wpv = (u32x4){lo.x, lo.y, hi.x, hi.y};
  }
  SB();
#define KMM(c, ks) do { \
      if (ks == 0 && (c) < 7) XWAIT(((c) + 1) & 7, kwait(c)); \
      if (ks == 0 && (c) == 3) RAWBAR(); \
      accK = MFMA32(fw[ks], as8(X[c][ks]), accK); PIN(accK); \
      if ((c) < 7) XT(((c) + 1) & 7, ks); \
      if ((c) < 7) fw[ks] = WF(0, 4 * (c) + 4 + ks); \
      SEQG(4 * (c) + 4 + ks); \
      SB(); } while (0)
#define KSTEP(c) do { KMM(c, 0); KMM(c, 1); KMM(c, 2); KMM(c, 3); } while (0)
  KSTEP(0); KSTEP(1); KSTEP(2); KSTEP(3); KSTEP(4); KSTEP(5); KSTEP(6); KSTEP(7);
  vmwait<3>();
  RAWBAR();
#pragma unroll
  for (int i = 0; i < 4; ++i) fw[i] = WF(32768, i);
  const u32 tag = epoch + 1u;
  bf16x8 wp0, wp1;
  {
    const u32 hm = (u32)(-((l >> 2) & 1));
    wp0 = as8(wpv & ~hm); wp1 = as8(wpv & hm);
  }
  u32 f0 = ~tag;
  {
    f32x16 P = MFMA32(wp0, cvt8(accK, 0), zero16);
    P = MFMA32(wp1, cvt8(accK, 1), P);
    PIN(P);
    u32 ph[16];
    auto vmm = [&](int m) NOPK {
      accV = MFMA32(fw[m & 3], as8(X[m >> 2][m & 3]), accV); PIN(accV);
      if (m + 4 < 32) fw[m & 3] = WF(32768, m + 4);
      if (m >= 2 && m < 10) LDW((m - 2) & 7, 0, -4096);
    };
    auto vput = [&](int pc) NOPK {
      u32x2 v = {pk2(accV[4 * pc], accV[4 * pc + 1]), pk2(accV[4 * pc + 2], accV[4 * pc + 3])};
      lds_w64(LXOR(vb, pc * 16), v);
    };
    auto pput = [&](int pc) NOPK {
      u32x4 v = {ph[4 * pc], ph[4 * pc + 1], ph[4 * pc + 2], ph[4 * pc + 3]};
      lds_w128(LXOR(pb, pc * 16), v);
    };
    vmm(0); vmm(1); vmm(2); vmm(3);
    feature_map(P, ph, [&](int m, auto& a, auto& c) NOPK {
      asm volatile("" : "+v"(a), "+v"(c));
      if (m < 8) { vmm(2 * m - 4); vmm(2 * m - 3); }
      else if (m < 28) vmm(m + 4);
      if (m == 24) { vmwait<0>(); RAWBAR(); put_flag(p.ws + WS_F1 + (rb * 16 + cb) * 128, tid, tag); }
      if (m == 27) pput(0);
      if (m == 28) pput(2);
      if (m == 29) { vput(0); vput(1); }
      if (m == 30) { vput(2); vput(3); }
      SB();
    });
    pput(1); pput(3);
  }
  BARRIER();
  bf16x8 fq[8];
  {
    f32x4 akv = {0, 0, 0, 0}, az = {0, 0, 0, 0};
    bf16x8 a[4], v[4];
    lptr pa1 = LXOR(pa0, 32), va1 = LXOR(va0, 32);
#pragma unroll
    for (int ks = 0; ks < 4; ++ks) {
      a[ks] = mk8(lds_tr(pa0 + ks * 4096), lds_tr(pa1 + 256 + ks * 4096));
      v[ks] = mk8(lds_tr(va0 + ks * 2048), lds_tr(va1 + 256 + ks * 2048));
      SB();
    }
    u32x4 ones = {0x3f803f80u, 0x3f803f80u, 0x3f803f80u, 0x3f803f80u};
    u32 zvo = (u32)(((l & 3) * 4 + G) * 8);
    asm volatile("" : "+v"(ones), "+v"(zvo));
#pragma unroll
    for (int ks = 0; ks < 4; ++ks) {
      akv = MFMA16(a[ks], v[ks], akv); PIN(akv);
      az = MFMA16(a[ks], as8(ones), az); PIN(az);
    }
#pragma unroll
    for (int i = 0; i < 4; ++i) fq[i] = WF(0, i);
    SB();
    u32x2 r0 = {nosent(pk2(akv[0], akv[1])), nosent(pk2(akv[2], akv[3]))}, r1 = {nosent(pk2(az[0], az[1])), nosent(pk2(az[2], az[3]))};
    gstore8_sc1<0>(kb, l * 8, r0);
    gstore8_sc1<512>(kb, zvo, r1);
  }
  SB();
#pragma unroll
  for (int i = 4; i < 8; ++i) fq[i] = WF(0, i);
  const u32 ovoff = (u32)((w * 8 + (l >> 3)) * 1024 + (cb & 7) * 128 + ((((l >> 1) & 3) ^ ((l >> 4) & 3)) * 32) + (l & 1) * 16);
  const int wr = (rb & 3) * 4;
#define LDWO(it) glds16(p.wo + ((((it) & 12) + wr) & 15) * 32768, ovoff + ((it) & 3) * 32768, smb + WO_AT(it))
  u32x2 R[16], Z;
  const char* kg = p.ws + WS_KVP + (cb * 4 + w) * 10240;
#define LDR(i) gload8_sc1<((i) & 3) * 640>(R[i], kg + ((i) >> 2) * 2560, l * 8)
  lptr kvf = sm + A_SC;
  lptr zer = sm + A_SC + 4608;
  u32x4 sel;
  lptr kd, ka_;
  u32 zgo, ovo;
  f32x16 accQ = zero16;
#define QMM(i) do { \
    accQ = MFMA32(fq[(i) & 7], as8(X[(i) >> 2][(i) & 3]), accQ); PIN(accQ); \
    if ((i) + 8 < 32) fq[(i) & 7] = WF(0, (i) + 8); \
    if ((i) == 11) RAWBAR(); \
    if ((i) == 21 && w == 0) asm volatile("global_load_dword %0, %1, %2 sc1" : "+v"(f0) : "v"(fvo0), "s"(p.ws + WS_F1) : "memory"); \
    if ((i) < 24 && !((i) & 1)) LDWO((i) < 12 ? 2 + ((i) >> 1) : ((i) < 16 ? ((i) - 12) >> 1 : ((i) >> 1))); \
    if ((i) == 1) { const u32 dd = (u32)((l & 15) - 4 * G); \
      const u32 one = 0x3f80u << ((dd & 1) * 16), s01 = dd < 2 ? one : 0u, s23 = (dd - 2) < 2 ? one : 0u; \
      sel = (u32x4){s01, s23, s01, s23}; asm volatile("" : "+v"(sel)); } \
    if ((i) == 3) { kd = kvf + (hh * 16 + (l & 15)) * 144 + sg * 32 + G * 8; asm volatile("" : "+v"(kd)); } \
    if ((i) == 5) { ka_ = ((r32 >> 4) == h) ? (kvf + r32 * 144) : zer; asm volatile("" : "+v"(ka_)); } \
    if ((i) == 7) { zgo = (u32)((l & 15) * 640 + G * 8); ovo = (u32)(((r32 >> 4) * 16) * 1024 + h * 256 + (l & 15) * 16); \
      asm volatile("" : "+v"(zgo), "+v"(ovo)); } \
    SB(); } while (0)
#define QMM4(i) do { QMM(i); QMM((i) + 1); QMM((i) + 2); QMM((i) + 3); } while (0)
  QMM4(0); QMM4(4); QMM4(8); QMM4(12); QMM4(16); QMM4(20); QMM4(24); QMM4(28);
  if (w == 0) {
    asm volatile("s_waitcnt vmcnt(1)" : "+v"(f0) :: "memory");
#pragma unroll 1
    for (u32 spins = 0; spins < SPIN_CAP && __builtin_amdgcn_ballot_w64(f0 != tag) != 0; ++spins)
      if (f0 != tag) asm volatile("global_load_dword %0, %1, %2 sc1\n\ts_waitcnt vmcnt(0)" : "=v"(f0) : "v"(fvo0), "s"(p.ws + WS_F1) : "memory");
  }
  RAWBAR();
#define RWAIT() asm volatile("s_waitcnt vmcnt(0)" : "+v"(Z), "+v"(R[0]), "+v"(R[1]), "+v"(R[2]), "+v"(R[3]), "+v"(R[4]), "+v"(R[5]), "+v"(R[6]), \
      "+v"(R[7]), "+v"(R[8]), "+v"(R[9]), "+v"(R[10]), "+v"(R[11]), "+v"(R[12]), "+v"(R[13]), "+v"(R[14]), "+v"(R[15]) :: "memory")
  auto reduce_kv = [&]() NOPK {
    RWAIT();
#pragma unroll 1
    for (u32 spins = 0; spins < SPIN_CAP; ++spins) {
      u32 m = umax3(Z.x, Z.y, 0);
#pragma unroll
      for (int i = 0; i < 16; ++i) m = umax3(m, R[i].x, R[i].y);
      if (__builtin_amdgcn_ballot_w64(m == SENT) == 0) break;
#pragma unroll 1
      for (; spins < SPIN_CAP && __builtin_amdgcn_ballot_w64(Z.x == SENT || Z.y == SENT) != 0; ++spins)
        if (Z.x == SENT || Z.y == SENT)
          asm volatile("global_load_dwordx2 %0, %1, %2 offset:512 sc1\n\ts_waitcnt vmcnt(0)" : "=v"(Z) : "v"(zgo), "s"(kg) : "memory");
      LDR(0); LDR(1); LDR(2); LDR(3); LDR(4); LDR(5); LDR(6); LDR(7);
      LDR(8); LDR(9); LDR(10); LDR(11); LDR(12); LDR(13); LDR(14); LDR(15);
      RWAIT();
    }
    f32x4 s = {0, 0, 0, 0};
#pragma unroll
    for (int i = 0; i < 16; i += 2) s = MFMA16(as8(sel), mk8(R[i], R[i + 1]), s);
    const float z0 = row_sum16(bflo(Z.x)), z1 = row_sum16(bfhi(Z.x)), z2 = row_sum16(bflo(Z.y)), z3 = row_sum16(bfhi(Z.y));
    u32x2 z = {pk2(z0, z1), pk2(z2, z3)};
    lds_w64(kd + 64, z);
    u32x2 a = {pk2(s[0], s[1]), pk2(s[2], s[3])};
    lds_w64(kd, a);
  };
  u32 pq[16];
  {
    if (tid < 8) { u32x4 z = {0, 0, 0, 0}; lds_w128(zer + tid * 16, z); }
    LDR(0); LDR(1); LDR(2); LDR(3); LDR(4); LDR(5); LDR(6); LDR(7);
    SB();
    f32x16 P = MFMA32(wp0, cvt8(accQ, 0), zero16);
    P = MFMA32(wp1, cvt8(accQ, 1), P);
    feature_map(P, pq, [&](int m, auto& a, auto& c) NOPK {
      if (m < 13) {
        asm volatile("" : "+v"(a), "+v"(c));
        if (m == 4) { LDR(8); LDR(9); }
        if (m == 5) { LDR(10); LDR(11); }
        if (m == 6) { LDR(12); LDR(13); }
        if (m == 7) { LDR(14); LDR(15); }
        if (m == 8) gload8_sc1<512>(Z, kg, zgo);
        if (m > 8) LDWO(m + 3);
        SB();
      }
    });
  }
#pragma unroll
  for (int i = 0; i < 16; ++i) asm volatile("" : "+v"(pq[i]));
  reduce_kv();
  BARRIER();
  {
    u32x4 ka[4], za[4];
#pragma unroll
    for (int ks = 0; ks < 4; ++ks) za[ks] = lds_r128(ka_ + 64 + ks * 16);
#pragma unroll
    for (int ks = 0; ks < 4; ++ks) ka[ks] = lds_r128(ka_ + ks * 16);
    SB();
    f32x16 num = zero16, den = zero16;
#pragma unroll
    for (int ks = 0; ks < 4; ++ks) {
      u32x4 bq = {pq[4 * ks], pq[4 * ks + 1], pq[4 * ks + 2], pq[4 * ks + 3]};
      den = MFMA32(as8(za[ks]), as8(bq), den);
    }
    PIN(den);
#pragma unroll
    for (int ks = 0; ks < 4; ++ks) {
      u32x4 bq = {pq[4 * ks], pq[4 * ks + 1], pq[4 * ks + 2], pq[4 * ks + 3]};
      num = MFMA32(as8(ka[ks]), as8(bq), num);
    }
    const float r0 = __builtin_amdgcn_rcpf(den[0] + 1e-6f), r1 = __builtin_amdgcn_rcpf(den[8] + 1e-6f);
    u32x4 o0, o1;
#pragma unroll
    for (int i = 0; i < 4; ++i) o0[i] = nosent(pk2(num[2 * i] * r0, num[2 * i + 1] * r0));
    gstore16_sc1<0>(ob, ovo, o0);
#pragma unroll
    for (int i = 0; i < 4; ++i) o1[i] = nosent(pk2(num[8 + 2 * i] * r1, num[9 + 2 * i] * r1));
    gstore16_sc1<512>(ob, ovo, o1);
  }
  SB();
  lptr wa[4];
#pragma unroll
  for (int t = 0; t < 4; ++t) wa[t] = sm + (16 * (G >> 1) + 4 * (G & 1) + q4) * 128 + ((t ^ (2 * (G & 1) + (q4 >> 1))) * 32) + p4 * 8;
  lptr wb[4];
#pragma unroll
  for (int t = 0; t < 4; ++t) { wb[t] = wa[t] + 32768; asm volatile("" : "+v"(wb[t])); }
#define WOF(i) (WO_AT((i) >> 2) >= 32768 ? tr8(wb[(i) & 3] + (WO_AT((i) >> 2) - 32768), 1024) : tr8(wa[(i) & 3] + WO_AT((i) >> 2), 1024))
  lptr os = sm + A_SC + 8192 + w * 2048;
  lptr osw = os + (l & 15) * 128 + (G & 1) * 8;
  lptr orp = os + (l >> 3) * 128 + (((l & 7) ^ (l >> 3)) * 16);
  u32 fvo = (u32)((l >> 3) * 2048 + (l & 7) * 16);
  u32 osx = (u32)(((G >> 1) ^ (l & 7)) * 16);
  asm volatile("" : "+v"(osw), "+v"(orp), "+v"(fvo), "+v"(osx));
  const char* og = p.ws + WS_OX + rt * 16384;
  f32x4 acc[4];
#pragma unroll 1
  for (u32 spins = 0; spins < SPIN_CAP; ++spins) {
    bf16x8 fo[32];
#pragma unroll
    for (int i = 0; i < 32; ++i) fo[i] = WOF(i);
    SB();
    u32 cv = l < 16 ? SENT : 0u;
#pragma unroll 1
    for (; spins < SPIN_CAP && __builtin_amdgcn_ballot_w64(cv == SENT) != 0; ++spins)
      if (cv == SENT) asm volatile("global_load_dword %0, %1, %2 offset:1020 sc1\n\ts_waitcnt vmcnt(0)" : "=v"(cv) : "v"(l * 1024), "s"(og) : "memory");
    u32x4 O[16];
#define LDO(i) gload16_sc1<((i) & 3) * 1024>(O[i], og + ((((i) & 12) + wr) & 15) * 1024, l * 16)
    LDO(0); LDO(1); LDO(2); LDO(3); LDO(4); LDO(5); LDO(6); LDO(7);
#pragma unroll
    for (int t = 0; t < 4; ++t) acc[t] = (f32x4){0, 0, 0, 0};
    u32 m = 0;
#define OSTEP(c) do { \
      asm volatile("s_waitcnt vmcnt(%1)" : "+v"(O[c]) : "i"((c) < 8 ? 7 : 15 - (c)) : "memory"); \
      _Pragma("unroll") for (int i = (c) * 4; i < (c) * 4 + 4; ++i) { \
        acc[i & 3] = MFMA16(fo[i & 31], as8(O[c]), acc[i & 3]); PIN(acc[i & 3]); \
        if (i < 32) fo[i] = WOF(i + 32); \
        if ((i & 3) == 0 && (c) < 8) LDO(((c) + 8) & 15); \
        if ((i & 3) == 1) { m = umax3(m, O[c].x, O[c].y); asm volatile("" : "+v"(m)); } \
        if ((i & 3) == 2) { m = umax3(m, O[c].z, O[c].w); asm volatile("" : "+v"(m)); } \
        SB(); } \
    } while (0)
    OSTEP(0); OSTEP(1); OSTEP(2); OSTEP(3); OSTEP(4); OSTEP(5); OSTEP(6); OSTEP(7);
    OSTEP(8); OSTEP(9); OSTEP(10); OSTEP(11); OSTEP(12); OSTEP(13); OSTEP(14); OSTEP(15);
    if (__builtin_amdgcn_ballot_w64(m == SENT) == 0) break;
  }
  {
#pragma unroll
    for (int t = 0; t < 4; ++t) {
      u32x2 v = {pk2(acc[t][0], acc[t][1]), pk2(acc[t][2], acc[t][3])};
      lds_w64(osw + (osx ^ (t * 32)), v);
    }
    CFENCE();
    char* op = p.out + (size_t)(rt * 16) * 2048 + (cb & 7) * 128;
    u32x4 v0 = lds_r128(orp), v1 = lds_r128(orp + 1024);
    gstore16_out<0>(op, fvo, v0);
    gstore16_out<0>(op + 8 * 2048, fvo, v1);
    if (b == 0 && tid == 0) gstore4_sc1(p.ws + WS_EPOCH_A, 0, tag);
  }
}

#define B_KC 0
#define B_UL 33280
#define B_YP 42496
#define B_TT 65536
#define B_KD 75264
#define B_TR 528
#define B_TC (53 * 528)

DI f32x2 cexp_pow(float xr, float xi, float pw) {
  const float m = __builtin_amdgcn_exp2f(xr * pw);
  const float a = __builtin_amdgcn_fractf(xi * pw);
  return (f32x2){m * __builtin_amdgcn_cosf(a), m * __builtin_amdgcn_sinf(a)};
}
DI f32x2 cmul(f32x2 z, f32x2 w) {
  return (f32x2){__builtin_fmaf(z.x, w.x, -z.y * w.y), __builtin_fmaf(z.x, w.y, z.y * w.x)};
}

DI void role_b(Params p, lptr sm, int b, const char* code, u32 code_len, u32 pf1) {

  const int tid = __builtin_amdgcn_workitem_id_x(), l = tid & 63;
  const int w = __builtin_amdgcn_readfirstlane(tid >> 6);
  const int g = b & 7, ii = b >> 3;
  const int d0 = g * 64 + ii * 2;
  __builtin_amdgcn_s_setprio(3);
  u32x4 ua, ub;
  {
    const char* up = p.u + (size_t)(ii * 64) * 1024 + g * 128;
    const u32 vo = (u32)((tid >> 3) * 1024 + (tid & 7) * 16);
    gload16_nt<0>(ua, up, vo); gload16_nt<0>(ub, up + 32768, vo);
  }
  u32 epoch, are, aim, bre, bim, cre, cim, ldt, dsk;
  {
    const u32 vo = (u32)(d0 * 256 + tid * 2);
    gload2_nt(ldt, p.ldt, (u32)((d0 + (tid >> 7)) * 2));
    gload2_nt(are, p.are, vo); gload2_nt(aim, p.aim, vo); gload2_nt(bre, p.bre, vo); gload2_nt(bim, p.bim, vo);
    gload2_nt(cre, p.cre, vo); gload2_nt(cim, p.cim, vo);
    gload4(dsk, p.dsk, (u32)(d0 * 2));
  }
  gload4_sc1(epoch, p.ws + WS_EPOCH_B + g * 256, 0);
  u32 pf = 0;
  {
    const u32 off = (u32)(ii * 6 + (l < 6 ? l : 0)) * 128;
    const char* pb = code;
    const u32 po = off + 4 <= code_len ? off : 0;
    gload4(pf, pb, po);
  }
  __builtin_amdgcn_s_setprio(0);
  const int i16 = l & 15, G = l >> 4, q4 = (l >> 2) & 3, p4 = l & 3;
  u32 kdr[11];
  {
    const int j0 = tid * 8 - 3;
#pragma unroll
    for (int e = 0; e < 11; ++e) {
      const int j = j0 + e;
      const int jb = (int)(pk2((float)j, 0.f) & 0xffffu) - 0x4300 + 128;
      kdr[e] = (u32)(j < 128 ? (j < 0 ? 688 : j) : jb) * 4;
      asm volatile("" : "+v"(kdr[e]));
    }
  }
  u32 kdw0, kdws0, kdws1;
  {
    const int c = (w & 1) * 16 + i16;
    const bool t1 = w >= 2;
    const int b0 = G < 2 ? (4 * G + 8 * c) : (G == 2 ? (256 + 4 * c) : (384 + 2 * c));
    const int b1 = G == 0 ? (512 + c) : (G == 1 ? (640 + c) : 672);
    kdw0 = (u32)(t1 ? b1 : b0) * 4;
    kdws0 = (u32)(t1 ? (G == 0 ? 32 : (G == 1 ? 32 - c : 0)) : 1) * 4;
    kdws1 = (u32)(t1 ? (G == 0 ? 64 : (G == 1 ? 32 - c : 0)) : (G == 3 ? 64 : 2)) * 4;
    asm volatile("" : "+v"(kdw0), "+v"(kdws0), "+v"(kdws1));
  }
  lptr ttr = sm + B_TT + (tid >> 3) * 128 + (((tid & 7) ^ ((tid >> 5) & 7)) * 16);
  lptr ttc = sm + B_TT + (tid & 15) * 512 + ((((tid >> 6) ^ (tid & 7))) * 16) + ((tid >> 4) & 3) * 4;
  const u32 xvo = (u32)((tid >> 4) * 8192 + (tid & 15) * 16);

  asm volatile("s_waitcnt vmcnt(10)" : "+v"(ua), "+v"(ub) : "v"(pf1) : "memory");
  __builtin_amdgcn_s_setprio(3);
  lds_w128(ttr, ua); lds_w128(ttr + 4096, ub);
  BARRIER();
  u32x4 ta, tc;
  {
    lptr t2 = (lptr)(uintptr_t)((u32)(uintptr_t)ttc ^ 64u);
#pragma unroll
    for (int j = 0; j < 4; ++j) { ta[j] = lds_r32(ttc + j * 128); tc[j] = lds_r32(t2 + j * 128); }
    char* tb = p.ws + WS_UT + g * 262144 + ii * 256;
    gstore16_sc1<0>(tb, xvo, ta);
    gstore16_sc1<0>(tb + 131072, xvo, tc);
  }
  asm volatile("s_waitcnt vmcnt(3)" : "+v"(epoch), "+v"(are), "+v"(aim), "+v"(bre), "+v"(bim), "+v"(cre), "+v"(cim), "+v"(ldt), "+v"(dsk) :: "memory");

  __builtin_amdgcn_s_setprio(0);
  const u32 tag = epoch + 1u;
  {
    const float dt = bflo(pk2(__builtin_amdgcn_exp2f(bflo(ldt) * 1.4426950408889634f), 0.f));
    const float xr = dt * bflo(are) * 1.4426950408889634f;
    const float xi = dt * bflo(aim) * 0.15915494309189535f;
    f32x2 z1 = cexp_pow(xr, -xi, 1.f);
    const f32x2 z8 = cexp_pow(xr, xi, 8.f);
    f32x2 c1 = cexp_pow(xr, xi, 64.f);
    f32x2 ra = cmul((f32x2){bflo(cre), -bflo(cim)}, (f32x2){dt * bflo(bre), -dt * bflo(bim)});
    f32x2 c2 = cmul(c1, c1);
    f32x2 c3 = cmul(c2, c1), z256 = cmul(c2, c2);
    z256.y = -z256.y;
    f32x2 c0 = {1.f, 0.f}, rb_ = ra, rc;
    asm volatile("s_mov_b32 m0, %0\n\ts_nop 0" :: "s"((u32)(uintptr_t)sm + (w >> 1) * B_TC + (w & 1) * 256) : "memory");
#define PUT(r, val) asm volatile("ds_write_addtid_b32 %0 offset:%1" :: "v"(val), "i"((r) * B_TR) : "memory")
#define PUTC(r, v) PUT(r, pk2((v).x, (v).y))
    PUT(21, 0x3f80u); PUTC(29, c1); PUTC(37, c2); PUTC(45, c3); PUTC(0, ra);
    constexpr int rowB[8] = {8, 12, 14, 16, 17, 18, 19, 20};
#pragma unroll
    for (int s = 1; s <= 8; ++s) {
      if (s == 3) PUTC(9, cmul(ra, z256));
      if (s == 5) { rc = cmul(ra, z256); PUTC(10, rc); }
      if (s == 7) PUTC(11, cmul(ra, z256));
      if (s == 6) { rc = cmul(rc, z256); PUTC(13, rc); }
      if (s == 8) { rc = cmul(rc, z256); PUTC(15, rc); }
      rb_ = cmul(rb_, z256); PUTC(rowB[s - 1], rb_);
      if (s < 8) {
        c0 = (s == 1) ? z8 : cmul(c0, z8); c1 = cmul(c1, z8); c2 = cmul(c2, z8); c3 = cmul(c3, z8); ra = cmul(ra, z1);
        PUTC(21 + s, c0); PUTC(29 + s, c1); PUTC(37 + s, c2); PUTC(45 + s, c3); PUTC(s, ra);
      }
      SB();
    }
  }
  if (tid == 0) lds_w32(sm + B_KD + 688 * 4, 0u);

  asm volatile("s_waitcnt vmcnt(0)" : "+v"(pf) :: "memory");

  BARRIER();
  __builtin_amdgcn_s_setprio(3);
  put_flag(p.ws + WS_FU + (g * 32 + ii) * 128, tid, tag);
  __builtin_amdgcn_s_setprio(0);

  {
    lptr ra = sm + ((w >> 1) * 16 + i16) * B_TR + G * 16;
    lptr ca = sm + (21 + (w & 1) * 16 + i16) * B_TR + G * 16;
    u32x4 fa[16], fb[16];
#pragma unroll
    for (int ks = 0; ks < 8; ++ks) {
      fa[ks] = lds_r128(ra + ks * 64); fb[ks] = lds_r128(ca + ks * 64);
      fa[8 + ks] = lds_r128(ra + B_TC + ks * 64); fb[8 + ks] = lds_r128(ca + B_TC + ks * 64);
    }
    SB();
    f32x4 k0 = {0, 0, 0, 0}, k1 = {0, 0, 0, 0};
#pragma unroll
    for (int ks = 0; ks < 8; ++ks) {
      k0 = MFMA16(as8(fa[ks]), as8(fb[ks]), k0);
      k1 = MFMA16(as8(fa[8 + ks]), as8(fb[8 + ks]), k1);
    }
    lptr kd = sm + B_KD;
    lds_w32(kd + kdw0, pk2(k0[0], k1[0]));
    lds_w32(kd + kdw0 + kdws0, pk2(k0[1], k1[1]));
    lds_w32(kd + kdw0 + kdws1, pk2(k0[2], k1[2]));
    lds_w32(kd + kdw0 + ((w >= 2 && G == 1) ? kdws0 : kdws0 + kdws1), pk2(k0[3], k1[3]));
  }
  BARRIER();

  {
    u32 kd[11];
#pragma unroll
    for (int e = 0; e < 11; ++e) kd[e] = lds_r32(sm + B_KD + kdr[e]);
    u32x4 z = {0, 0, 0, 0};
    if (tid < 16) lds_w128(sm + B_KC + (tid >> 1) * 4160 + (tid & 1) * 16, z);
    if (tid >= 64 && tid < 128) lds_w128(sm + B_UL + ((tid - 64) >> 5) * 4608 + (tid & 31) * 16, z);
    u32 lo[10], hi[10];
#pragma unroll
    for (int e = 0; e < 10; ++e) {
      lo[e] = __builtin_amdgcn_perm(kd[e + 1], kd[e], 0x05040100u);
      hi[e] = __builtin_amdgcn_perm(kd[e + 1], kd[e], 0x07060302u);
    }
    lptr kc = sm + B_KC + 32 + tid * 16;
#pragma unroll
    for (int q = 0; q < 4; ++q) {
      u32x4 a = {lo[3 - q], lo[5 - q], lo[7 - q], lo[9 - q]}, c = {hi[3 - q], hi[5 - q], hi[7 - q], hi[9 - q]};
      lds_w128(kc + q * 4160, a);
      lds_w128(kc + 16640 + q * 4160, c);
    }
  }

  __builtin_amdgcn_s_setprio(3);
  if (w == 0) poll_flags(p.ws + WS_FU + g * 4096, l, 31, tag);
  BARRIER();

  u32x4 U0, U1;
  const int cch = w >> 1, rh = w & 1;
  lptr ka = sm + B_KC + cch * 16640 + q4 * 4160 + 2 * (16 + 16 - 8 * G - 4 + 4 * p4) + rh * 256;
  lptr uap = sm + B_UL + cch * 4608 + 2 * (256 + 16 * i16 - 16 + 8 * G) - rh * 256;
  bf16x8 A[8], B[8];
  {
    const char* tb = p.ws + WS_UT + g * 262144 + ii * 8192;
    gload16_sc1<0>(U0, tb, tid * 16);
    gload16_sc1<0>(U1, tb + 4096, tid * 16);
#pragma unroll
    for (int a = 0; a < 8; ++a) A[a] = mk8(lds_tr(ka + a * 512 + 8), lds_tr(ka + a * 512));
    SB();
    asm volatile("s_waitcnt vmcnt(0)" : "+v"(U0), "+v"(U1) :: "memory");
    lptr ul = sm + B_UL + 512 + tid * 8;
    u32x2 a = {__builtin_amdgcn_perm(U0.y, U0.x, 0x05040100u), __builtin_amdgcn_perm(U0.w, U0.z, 0x05040100u)};
    u32x2 c = {__builtin_amdgcn_perm(U0.y, U0.x, 0x07060302u), __builtin_amdgcn_perm(U0.w, U0.z, 0x07060302u)};
    lds_w64(ul, a); lds_w64(ul + 4608, c);
    a = (u32x2){__builtin_amdgcn_perm(U1.y, U1.x, 0x05040100u), __builtin_amdgcn_perm(U1.w, U1.z, 0x05040100u)};
    c = (u32x2){__builtin_amdgcn_perm(U1.y, U1.x, 0x07060302u), __builtin_amdgcn_perm(U1.w, U1.z, 0x07060302u)};
    lds_w64(ul + 2048, a); lds_w64(ul + 4608 + 2048, c);
  }
  BARRIER();

  __builtin_amdgcn_s_setprio(0);
  {
    f32x4 acc[8];
#pragma unroll
    for (int t = 0; t < 8; ++t) acc[t] = (f32x4){0, 0, 0, 0};
#pragma unroll
    for (int a = 7; a >= 0; --a) B[a] = as8(lds_r128(uap + a * 512));
    SB();
#pragma unroll 1
    for (int r = 0; r < 3; ++r) {
      ka += 64; uap -= 64;
#pragma unroll
      for (int a = 0; a < 8; ++a) {
#pragma unroll
        for (int be = 7 - a; be >= 0; --be) { acc[a + be] = MFMA16(A[a], B[be], acc[a + be]); PIN(acc[a + be]); }
        A[a] = mk8(lds_tr(ka + a * 512 + 8), lds_tr(ka + a * 512));
        B[7 - a] = as8(lds_r128(uap + (7 - a) * 512));
        SB();
      }
    }
    lptr yp = sm + B_YP + w * 8192 + i16 * 64 + ((G ^ ((i16 >> 1) & 3)) * 16);
#pragma unroll
    for (int a = 0; a < 8; ++a) {
#pragma unroll
      for (int be = 7 - a; be >= 0; --be) { acc[a + be] = MFMA16(A[a], B[be], acc[a + be]); PIN(acc[a + be]); }
      if (a > 0) lds_w128(yp + (a - 1) * 1024, __builtin_bit_cast(u32x4, acc[a - 1]));
      SB();
    }

    __builtin_amdgcn_s_setprio(3);
    lds_w128(yp + 7 * 1024, __builtin_bit_cast(u32x4, acc[7]));
  }
  BARRIER();
  {
    const float D0 = bflo(dsk), D1 = bfhi(dsk);
    lptr yr = sm + B_YP + (tid >> 2) * 64 + (((tid & 3) ^ ((tid >> 3) & 3)) * 16);
    char* yb = p.ws + WS_UT + g * 262144 + ii * 8192;
    u32x4 Y[8];
#pragma unroll
    for (int k = 0; k < 8; ++k) Y[k] = lds_r128(yr + (k & 1) * 4096 + (k >> 1) * 8192);
    SB();
#pragma unroll
    for (int gp = 0; gp < 2; ++gp) {
      f32x4 y0 = __builtin_bit_cast(f32x4, Y[gp]) + __builtin_bit_cast(f32x4, Y[2 + gp]);
      f32x4 y1 = __builtin_bit_cast(f32x4, Y[4 + gp]) + __builtin_bit_cast(f32x4, Y[6 + gp]);
      const u32x4 U = gp ? U1 : U0;
      u32x4 v;
#pragma unroll
      for (int j = 0; j < 4; ++j) v[j] = ~(U[j] ^ nosent(pk2(__builtin_fmaf(bflo(U[j]), D0, y0[j]), __builtin_fmaf(bfhi(U[j]), D1, y1[j]))));
      gstore16_sc1<0>(yb + gp * 4096, tid * 16, v);
    }
  }

  {
    u32x4 a, c;
    const char* tb = p.ws + WS_UT + g * 262144 + ii * 256;
    if (ii == 0 && tid == 0) gstore4_sc1(p.ws + WS_EPOCH_B + g * 256, 0, tag);
    __builtin_amdgcn_s_sleep(32);
    bool wa = true, wc = true;
#pragma unroll 1
    for (u32 spins = 0; spins < SPIN_CAP; ++spins) {
      if (wa | wc) {
        asm volatile("global_load_dwordx4 %0, %2, %3 sc1\n\tglobal_load_dwordx4 %1, %2, %4 sc1\n\ts_waitcnt vmcnt(0)"
                     : "=&v"(a), "=&v"(c) : "v"(xvo), "s"(tb), "s"(tb + 131072) : "memory");
        a = ~(a ^ ta); c = ~(c ^ tc);
        wa = umax3(umax3(a.x, a.y, a.z), a.w, 0) == SENT;
        wc = umax3(umax3(c.x, c.y, c.z), c.w, 0) == SENT;
      }
      if (__builtin_amdgcn_ballot_w64(wa | wc) == 0) break;
      __builtin_amdgcn_s_sleep(16);
    }

    RAWBAR();
    lptr t2 = (lptr)(uintptr_t)((u32)(uintptr_t)ttc ^ 64u);
#pragma unroll
    for (int j = 0; j < 4; ++j) { lds_w32(ttc + j * 128, a[j]); lds_w32(t2 + j * 128, c[j]); }
    BARRIER();
    u32x4 v0 = lds_r128(ttr), v1 = lds_r128(ttr + 4096);
    char* op = p.out + (size_t)(ii * 64) * 2048 + 1024 + g * 128;
    const u32 vo = (u32)((tid >> 3) * 2048 + (tid & 7) * 16);
    gstore16_out<0>(op, vo, v0);
    gstore16_out<0>(op + 65536, vo, v1);
    asm volatile(".Lpf_end:");
  }

}

__global__ void __launch_bounds__(256, 2) NOPK fused_kernel(Params p, Touch tt) {
  __shared__ __attribute__((aligned(256))) char smem[81920];
  lptr sm = (lptr)smem;
  const char* code;
  u32 code_len;
  asm volatile("s_getpc_b64 %0\n.Lpf_base:\n\ts_mov_b32 %1, .Lpf_end-.Lpf_base" : "=s"(code), "=s"(code_len));
  const u32 bid = __builtin_amdgcn_workgroup_id_x();
  u32 pf = 0;
  {
    const u32 k = (bid >> 3) & 31;
    const char* base = tt.a[k];
    if (k == 31) base = code;
    if (__builtin_amdgcn_workitem_id_x() == 0) gload4_sc1(pf, base, 0);
  }
  asm volatile("" : "+s"(p.x), "+s"(p.wqkv), "+s"(p.wphi), "+s"(p.wo), "+s"(p.u), "+s"(p.are), "+s"(p.aim), "+s"(p.bre), "+s"(p.bim),
               "+s"(p.cre), "+s"(p.cim), "+s"(p.ldt), "+s"(p.dsk), "+s"(p.out), "+s"(p.ws));
  if (bid < 256) role_b(p, sm, bid, code, code_len, pf);
  else role_a(p, sm, bid - 256, pf);
}

extern "C" void kernel_launch(void* const* d_in, const int* in_sizes, int n_in, void* d_out, int out_size, void* d_ws,
                              size_t ws_size, hipStream_t stream) {
  Params p;
  p.x = (const char*)d_in[0]; p.wqkv = (const char*)d_in[1]; p.wphi = (const char*)d_in[2]; p.wo = (const char*)d_in[3];
  p.u = (const char*)d_in[4]; p.are = (const char*)d_in[5]; p.aim = (const char*)d_in[6]; p.bre = (const char*)d_in[7];
  p.bim = (const char*)d_in[8]; p.cre = (const char*)d_in[9]; p.cim = (const char*)d_in[10]; p.ldt = (const char*)d_in[11];
  p.dsk = (const char*)d_in[12];
  p.out = (char*)d_out; p.ws = (char*)d_ws;
  Touch tt;
  for (int i = 0; i < 13; ++i) tt.a[i] = (const char*)d_in[i];
  tt.a[13] = p.out; tt.a[14] = p.out + 2097152; tt.a[15] = p.out + 4194300;
  tt.a[16] = p.ws; tt.a[17] = p.ws + WS_OX; tt.a[18] = p.ws + WS_UT; tt.a[19] = p.ws + WS_UT + 1048576; tt.a[20] = p.ws + WS_UT + 2097148;
  tt.a[21] = p.x + 2097148; tt.a[22] = p.u + 2097148; tt.a[23] = p.wqkv + 1572860; tt.a[24] = p.wo + 524284;
  tt.a[25] = p.x + 1048576; tt.a[26] = p.u + 1048576; tt.a[27] = p.wqkv + 524288; tt.a[28] = p.wqkv + 1048576;
  tt.a[29] = p.x + 524288; tt.a[30] = p.x + 1572864; tt.a[31] = p.wqkv;
  fused_kernel<<<512, 256, 0, stream>>>(p, tt);
}
```
